```python
import torch

def _softmax(x, axis=-1):
    return torch.softmax(x, dim=axis)
def _gelu(x):
    return torch.nn.functional.gelu(x, approximate="tanh")
def _silu(x):
    return torch.nn.functional.silu(x)
def _sigmoid(x):
    return torch.sigmoid(x)
def _layernorm(x, axis=-1, eps=1e-5):
    m = x.mean(dim=axis, keepdim=True)
    v = x.var(dim=axis, keepdim=True, unbiased=False)
    return (x - m) / torch.sqrt(v + eps)
def _scatter_add(tgt, idx, src, axis=0):
    return tgt.clone().index_add_(axis, idx, src)
def _segment_sum(src, seg, n):
    out = torch.zeros((n,) + src.shape[1:], dtype=src.dtype)
    return out.index_add_(0, seg, src)
def _pad(x, pw):
    flat = tuple(v for pair in reversed(pw) for v in pair)
    return torch.nn.functional.pad(x, flat)

kernel_name = 'standalone__detection_seg_dino__vision'

def setup_inputs():

    _w = (lambda a: (lambda t: t.to(torch.bfloat16) if t.is_floating_point() else t)(torch.as_tensor(a)))
    H = W = (224 // 2) * 2
    C = 192
    Nq = 16
    sc = C ** -0.5
    return {
        "q": _w((lambda *s: torch.randn(*s, dtype=torch.bfloat16) * 0.05)(1, Nq, C)),
        "f0": _w((lambda *s: torch.randn(*s, dtype=torch.bfloat16) * 0.8)(1, H, W, C)),
        "f1": _w((lambda *s: torch.randn(*s, dtype=torch.bfloat16) * 0.8)(1, H // 2, W // 2, C)),
        "wq": _w((lambda *s: torch.randn(*s, dtype=torch.bfloat16) * 1.6)(C, C) * sc),
        "wk": _w((lambda *s: torch.randn(*s, dtype=torch.bfloat16) * 1.6)(C, C) * sc),
        "wv": _w((lambda *s: torch.randn(*s, dtype=torch.bfloat16) * 0.8)(C, C) * sc),
        "wo": _w((lambda *s: torch.randn(*s, dtype=torch.bfloat16) * 0.8)(C, C) * sc),
    }

def reference(q, f0, f1, wq, wk, wv, wo):
    B, H, W, C = f0.shape
    f1u = (lambda x, n, axis: torch.repeat_interleave(x, n, dim=axis))((lambda x, n, axis: torch.repeat_interleave(x, n, dim=axis))(f1, 2, axis=1), 2, axis=2)
    fused = f0 + f1u
    feats = _layernorm(fused.reshape(B, H * W, C), axis=-1)
    qn = _layernorm(q, axis=-1)
    Q = torch.einsum("bnc,cd->bnd", qn, wq)
    K = torch.einsum("bmc,cd->bmd", feats, wk)
    V = torch.einsum("bmc,cd->bmd", feats, wv)
    scale = C ** -0.5
    logits = torch.einsum("bnd,bmd->bnm", Q, K) * scale
    attn = _softmax(logits, axis=-1)
    ctx = torch.einsum("bnm,bmd->bnd", attn, V)
    return q + torch.einsum("bnd,dc->bnc", ctx, wo)
```

```cpp
#include <hip/hip_runtime.h>
typedef unsigned short u16; typedef unsigned u32; typedef unsigned long long u64;
typedef short s4 __attribute__((ext_vector_type(4)));
typedef short s8 __attribute__((ext_vector_type(8)));
typedef float f2 __attribute__((ext_vector_type(2)));
typedef float f4 __attribute__((ext_vector_type(4)));
typedef unsigned v2u __attribute__((ext_vector_type(2)));
typedef unsigned v4u __attribute__((ext_vector_type(4)));
typedef __bf16 b2 __attribute__((ext_vector_type(2)));
#define GAS __attribute__((address_space(1)))
#define LAS __attribute__((address_space(3)))
#define DEVI __device__ __forceinline__

constexpr int C = 192, NP = 224;
constexpr u32 F0B = 50176u * 384u, F1B = 12544u * 384u, WB = 192u * 384u;
constexpr u32 WS_FLAG = 256, WS_QK = 4096, WS_WVO = 16384, WS_M = 90112, WS_L = 118784, WS_Y = 147456, WS_END = WS_Y + 64u * NP * 128u;
constexpr float QSCALE = 0.07216878364870322f * 1.4426950408889634f;
constexpr float EPS = 1e-5f;
constexpr int SPIN = 1 << 16;
#define CODE_KB 13
#define QSLEEP 2
#define RSLEEP 1
constexpr u32 L_F0 = 0, L_F1 = 98304, L_QK = 122880, L_PF0 = 129024, L_PF1 = 137216, L_AUX = 141312, L_M = 143360, L_O = 143616, L_JUNK = 149760, L_PRE = 150016, L_TOTAL = 151040;

DEVI f4 mfma(v4u a, v4u b, f4 c) { return __builtin_amdgcn_mfma_f32_16x16x32_bf16(__builtin_bit_cast(s8, a), __builtin_bit_cast(s8, b), c, 0, 0, 0); }
DEVI u32 pk(float lo, float hi) { f2 p = {lo, hi}; return __builtin_bit_cast(u32, __builtin_convertvector(p, b2)); }
DEVI float blo(u32 x) { return __uint_as_float(x << 16); }
DEVI float bhi(u32 x) { return __uint_as_float(x & 0xffff0000u); }
DEVI v4u lds128(u32 a) { return *(LAS v4u*)a; }
DEVI v2u ldstr(u32 a) { return __builtin_bit_cast(v2u, __builtin_amdgcn_ds_read_tr16_b64_v4i16((LAS s4*)a)); }
DEVI v4u cat(v2u a, v2u b) { v4u r = {a.x, a.y, b.x, b.y}; return r; }
template <int CTRL> DEVI float dpp(float x) { return __int_as_float(__builtin_amdgcn_mov_dpp(__float_as_int(x), CTRL, 0xf, 0xf, true)); }
DEVI float xg_sum(float x) {
  auto s = __builtin_amdgcn_permlane16_swap(__float_as_uint(x), __float_as_uint(x), false, false);
  x = __uint_as_float(s[0]) + __uint_as_float(s[1]);
  auto t = __builtin_amdgcn_permlane32_swap(__float_as_uint(x), __float_as_uint(x), false, false);
  return __uint_as_float(t[0]) + __uint_as_float(t[1]);
}
DEVI float vmax(float a, float b) { float r; asm("v_max_f32 %0, %1, %2" : "=v"(r) : "v"(a), "v"(b)); return r; }
DEVI float row_sum16(float x) { x += dpp<0x128>(x); x += dpp<0x124>(x); x += dpp<0x122>(x); x += dpp<0x121>(x); return x; }
DEVI float bperm(int srclane, float x) { return __int_as_float(__builtin_amdgcn_ds_bpermute(srclane * 4, __float_as_int(x))); }
DEVI v4u mkrs(const void* p, u32 bytes) { u64 a = (u64)p; v4u r = {(u32)a, (u32)(a >> 32) & 0xffffu, bytes, 0x00020000u}; return r; }
DEVI void dma16(v4u rs, u32 voff, u32 soff, u32 ldsa) {
  asm volatile("s_mov_b32 m0, %3\n\ts_nop 0\n\tbuffer_load_dwordx4 %0, %1, %2 offen lds" :: "v"(voff), "s"(rs), "s"(soff), "s"(ldsa) : "memory");
}
DEVI void dma_tile(v4u rs, u32 va, u32 vb, u32 s0, u32 ms, u32 l0) {
#pragma unroll
  for (int j = 0; j < 3; ++j) { dma16(rs, va, s0 + j * ms, l0 + j * 2048); dma16(rs, vb, s0 + j * ms, l0 + j * 2048 + 1024); }
}
#define WAITV(n) asm volatile("s_waitcnt vmcnt(" #n ")" ::: "memory")
#define BAR() do { asm volatile("s_waitcnt lgkmcnt(0)" ::: "memory"); __builtin_amdgcn_s_barrier(); asm volatile("" ::: "memory"); } while (0)
#define POLL_LOOP _Pragma("unroll 1") for (int it_ = 0; it_ < SPIN; ++it_)
#define FENCE() asm volatile("" ::: "memory")
#define SB() __builtin_amdgcn_sched_barrier(0)
#define PIN(x) asm volatile("" : "+v"(x))

#define STAMP(k) do {} while (0)
#define STAMP_DECL
#define STAMP_ARG
#define STAMP_PASS
DEVI u32 req_epoch(const char* ws) { u32 e; asm volatile("s_load_dword %0, %1, 0x0" : "=s"(e) : "s"(ws) : "memory"); return e; }
DEVI u32 get_tag(u32 e) { u32 t; asm volatile("s_waitcnt lgkmcnt(0)\n\ts_add_u32 %0, %1, 1" : "=s"(t) : "s"(e) : "memory", "scc"); return t; }
DEVI char* karg(u32 idx) { u64 p; asm volatile("s_load_dwordx2 %0, %1, %2\n\ts_waitcnt lgkmcnt(0)" : "=s"(p) : "s"(__builtin_amdgcn_kernarg_segment_ptr()), "s"(idx * 8u) : "memory"); return (char*)p; }
#define WRS(ws) __builtin_amdgcn_make_buffer_rsrc((ws), 0, WS_END, 0x00020000)

DEVI void helper_qk(int h, char* ws, u32 tag, u32 lds STAMP_ARG) {
  const int tid = threadIdx.x, lane = tid & 63, w = __builtin_amdgcn_readfirstlane(tid >> 6), g = lane >> 4, i = lane & 15;
  const int qq = i >> 2;
  u32 ta = lds + (g >> 1) * 6144 + (w >> 2) * 2048 + (8 * (g & 1) + qq) * 128 + (((w & 3) ^ (((qq >> 1) & 1) | ((g & 1) << 1))) << 5) + (i & 3) * 8;
  u32 na = lds + L_QK + lane * 16;
  const u32 fo = (16 * (g >> 1) + i) * 16;
  u32 fw = lds + L_O + w * 512 + fo + (g & 1) * 8;
  PIN(ta); PIN(na); PIN(fw);
  if (w < 4) {
    u32 qa = lds + L_F1 + w * 2048 + g * 384 + i * 8;
    u32 nw = lds + L_QK + (i >> 3) * 1024 + (16 * ((i >> 1) & 3) + 4 * w + g) * 16 + (i & 1) * 8;
    PIN(qa); PIN(nw);
    WAITV(12);
    STAMP(3);
    const v2u q0 = *(LAS v2u*)qa, q1 = *(LAS v2u*)(qa + 128), q2 = *(LAS v2u*)(qa + 256);
    float x[12] = {blo(q0.x), bhi(q0.x), blo(q0.y), bhi(q0.y), blo(q1.x), bhi(q1.x), blo(q1.y), bhi(q1.y), blo(q2.x), bhi(q2.x), blo(q2.y), bhi(q2.y)};
    const float mu = row_sum16((((x[0] + x[1]) + (x[2] + x[3])) + ((x[4] + x[5]) + (x[6] + x[7]))) + ((x[8] + x[9]) + (x[10] + x[11]))) * (1.f / C);
    float v = 0.f;
#pragma unroll
    for (int j = 0; j < 12; ++j) { x[j] -= mu; v = __builtin_fmaf(x[j], x[j], v); }
    const float r = __builtin_amdgcn_rsqf(row_sum16(v) * (1.f / C) + EPS);
#pragma unroll
    for (int j = 0; j < 3; ++j) { const v2u o = {pk(x[4 * j] * r, x[4 * j + 1] * r), pk(x[4 * j + 2] * r, x[4 * j + 3] * r)}; *(LAS v2u*)(nw + j * 2048) = o; }
    STAMP(4);
  }
  BAR();
  v4u y[6], x[6];
#pragma unroll
  for (int s = 0; s < 6; ++s) y[s] = lds128(na + s * 1024);
  if (w < 4) WAITV(6); else WAITV(0);
  STAMP(5);
  BAR();
  STAMP(1);
  f4 a = {0, 0, 0, 0};
#pragma unroll
  for (int s = 0; s < 6; ++s) x[s] = cat(ldstr(ta + s * 12288), ldstr(ta + s * 12288 + 512));
  SB();
#pragma unroll
  for (int s = 0; s < 6; ++s) a = mfma(x[s], y[s], a);
  { const v2u o = {pk(a[0], a[1]), pk(a[2], a[3])}; *(LAS v2u*)fw = o; }
  if (w < 4) {
    const int T = 4 * h + w;
    u32 so = WS_QK + ((T >> 1) * 2 + (g & 1)) * 1024 + (T & 1) * 512 + fo;
    const u32 ka = lds + (12 + w) * 6144 + i * 128 + ((g ^ (i & 6)) << 4);
    WAITV(0);
#pragma unroll
    for (int s = 0; s < 6; ++s) x[s] = lds128((ka ^ ((s & 1) << 6)) + (s >> 1) * 2048);
    PIN(so);
    BAR();
    STAMP(6);
#pragma unroll
    for (int s = 0; s < 6; ++s) y[s] = lds128(na + (L_O - L_QK) + s * 1024);
    SB();
    f4 c = {0, 0, 0, 0};
#pragma unroll
    for (int s = 0; s < 6; ++s) c = mfma(x[s], y[s], c);
    c *= QSCALE;
    const v4u o = {pk(c[0], c[1]), tag, pk(c[2], c[3]), tag};
    __builtin_amdgcn_raw_buffer_store_b128(o, WRS(ws), so, 0, 16);
  } else BAR();
}
DEVI void helper_wvo(int T, char* ws, u32 tag, u32 lds STAMP_ARG) {
  const int tid = threadIdx.x, lane = tid & 63, w = __builtin_amdgcn_readfirstlane(tid >> 6), g = lane >> 4, i = lane & 15;
  const int qq = i >> 2;
  u32 tb = lds + 73728 + (8 * g + qq) * 128 + (((T & 3) ^ (((qq >> 1) & 1) | ((g & 1) << 1))) << 5) + (i & 3) * 8;
  u32 aa = lds + 2 * w * 6144 + i * 128 + ((g ^ (i & 6)) << 4), aax = aa ^ 64;
  u32 ca = lds + L_PF0 + i * 32;
  PIN(tb); PIN(aa); PIN(aax); PIN(ca);
  WAITV(0); BAR();
  STAMP(1);
  f4 a0 = {0, 0, 0, 0}, a1 = {0, 0, 0, 0};
  if (w < 6) {
    v4u b[6], x0[6], x1[6];
#pragma unroll
    for (int u = 0; u < 6; ++u) {
      b[u] = cat(ldstr(tb + u * 4096), ldstr(tb + u * 4096 + 512));
      const u32 ao = ((u & 1) ? aax : aa) + (u >> 1) * 2048;
      x0[u] = lds128(ao); x1[u] = lds128(ao + 6144);
    }
    SB();
#pragma unroll
    for (int u = 0; u < 6; ++u) { a0 = mfma(x0[u], b[u], a0); a1 = mfma(x1[u], b[u], a1); }
    *(LAS float*)(ca + w * 4) = xg_sum(((a0[0] + a0[1]) + (a0[2] + a0[3])) + ((a1[0] + a1[1]) + (a1[2] + a1[3])));
  }
  BAR();
  if (w < 6) {
    const f4 c0 = *(LAS f4*)ca; const f2 c1 = *(LAS f2*)(ca + 16);
    const float mean = (((c0[0] + c0[1]) + (c0[2] + c0[3])) + (c1[0] + c1[1])) * (1.f / C);
    const v4u o = {pk(a0[0] - mean, a0[1] - mean), pk(a0[2] - mean, a0[3] - mean), pk(a1[0] - mean, a1[1] - mean), pk(a1[2] - mean, a1[3] - mean)};
    __builtin_amdgcn_raw_buffer_store_b128(o, WRS(ws), WS_WVO + (T * 6 + w) * 1024 + lane * 16, 0, 16);
  }
  WAITV(0); BAR();
  if (tid == 0) { const v2u fl = {tag, ~tag}; __builtin_amdgcn_raw_buffer_store_b64(fl, WRS(ws), WS_FLAG + T * 8, 0, 16); }
}

struct Stat { float r, nrm, sig; };
DEVI Stat token_stats(f4 gg, int i) {
  float sel = (i & 1) ? gg[1] : gg[0]; const float sel2 = (i & 1) ? gg[3] : gg[2]; sel = (i & 2) ? sel2 : sel;
  const float ss = bperm(16 * (i >> 2) + i, sel), sx = bperm(48 + i, gg[3]);
  const float mu = sx * (1.f / C);
  const float ve = vmax(__builtin_fmaf(-mu, mu, __builtin_fmaf(ss, 1.f / C, EPS)), EPS);
  Stat s; s.r = __builtin_amdgcn_rsqf(ve); s.nrm = -s.r * mu; s.sig = ve * s.r; return s;
}
DEVI f4 logits(f4 sa, f4 cb, float r) { f4 lg;
#pragma unroll
  for (int k = 0; k < 4; ++k) lg[k] = __builtin_fmaf(r, sa[k], cb[k]);
  return lg; }
DEVI void fold_max(f4 m, int i, u32 ma) {
  const bool o1 = i & 1, o2 = i & 2;
  const float x01 = vmax(o1 ? m[1] : m[0], dpp<0xB1>(o1 ? m[0] : m[1])), x23 = vmax(o1 ? m[3] : m[2], dpp<0xB1>(o1 ? m[2] : m[3]));
  const float x = vmax(o2 ? x23 : x01, dpp<0x4E>(o2 ? x01 : x23));
  __builtin_amdgcn_ds_fmaxf((LAS float*)ma, x, 0, 0, false);
}
DEVI void write_aux(Stat st, u32 wxa) {
  const u32 sb = __float_as_uint(st.sig), sh = sb & 0xffff0000u;
  *(LAS u32*)wxa = __builtin_amdgcn_perm(__float_as_uint(st.sig - __uint_as_float(sh)), sb, 0x07060302u);
}
DEVI void write_p(f4 lg, f4 M, float r, u32 w0a, u32 w1a) {
  float w[4], w2[4];
#pragma unroll
  for (int k = 0; k < 4; ++k) w[k] = __builtin_amdgcn_exp2f(lg[k] - M[k]) * r;
  const v2u o = {pk(w[0], w[1]), pk(w[2], w[3])};
  *(LAS v2u*)w0a = o;
#pragma unroll
  for (int k = 0; k < 4; ++k) w2[k] = w[k] + dpp<0xB1>(w[k]);
  const v2u o2 = {pk(w2[0], w2[1]), pk(w2[2], w2[3])};
  *(LAS v2u*)w1a = o2;
}
DEVI void qk_request(v4u wsr, int v, int lane, u32 lds) {
  const u32 so = WS_QK + v * 3072, la = lds + L_PF0 + v * 3072;
  asm volatile("s_mov_b32 m0, %3\n\ts_nop 0\n\tbuffer_load_dwordx4 %0, %1, %2 offen sc1 lds\n\tbuffer_load_dwordx4 %0, %1, %2 offen offset:1024 sc1 lds\n\tbuffer_load_dwordx4 %0, %1, %2 offen offset:2048 sc1 lds"
               :: "v"(lane * 16), "s"(wsr), "s"(so), "s"(la) : "memory");
}
#define RBAR() __builtin_amdgcn_s_barrier()
DEVI u32 streamer(int sidx, v4u r0, u32 s0, u32 va, u32 vb, char* ws, u32 lds STAMP_ARG) {
  const int tid = threadIdx.x, lane = tid & 63, w = __builtin_amdgcn_readfirstlane(tid >> 6), g = lane >> 4, i = lane & 15;
  const v4u wsr = mkrs(ws, WS_END);
  const __amdgpu_buffer_rsrc_t wrs = WRS(ws);
  const int qq = i >> 2, p = i & 3, t4 = 4 * g + qq;
  const u32 tag = get_tag(req_epoch(ws));
  u32 qka = lds + L_QK + lane * 16, mi = lds + L_M + i * 4, pa0 = lds + L_PF0 + t4 * 32 + p * 8;
  if (w < 8) {
    const bool A = w < 4;
    if (A) {
      if (lane < 48) { const v4u ones = {0x3f803f80u, 0x3f803f80u, 0x3f803f80u, 0x3f803f80u}; *(LAS v4u*)(lds + L_F1 + (lane >= 24 ? 12288 - 3072 : 0) + w * 3072 + (lane >> 3) * 1024 + 896 + (lane & 7) * 16) = ones; }
      if (tid < 16) *(LAS float*)(lds + L_M + tid * 4) = -INFINITY;
      WAITV(6);
    }
    BAR();
    if (A) { dma_tile(r0, va, vb, s0 + 224 * 384, 128, lds + (8 + w) * 6144); RBAR(); }
    u32 ab1 = lds + L_F1 + w * 3072 + (i >> 1) * 128 + ((g ^ ((i >> 1) & 6) ^ ((w & 1) << 2)) << 4), ab1x = ab1 ^ 64;
    u32 aa = lds + L_F0 + w * 6144 + i * 128 + ((g ^ (i & 6)) << 4), aax = aa ^ 64;
    const float bias = i >= 14 ? -INFINITY : 0.f;
    const u32 dbl = i >= 14 ? 0u : 0x00800080u;
    v4u b1[6]; f4 g1 = {0, 0, 0, 0};
#pragma unroll
    for (int s = 0; s < 6; ++s) b1[s] = lds128(((s & 1) ? ab1x : ab1) + (s >> 1) * 1024);
    SB();
#pragma unroll
    for (int s = 0; s < 6; ++s) { g1 = mfma(b1[s], b1[s], g1); b1[s] += dbl; }
    u32 ma = lds + L_M + (4 * g + (i & 3)) * 4, mr = lds + L_M + g * 16;
    u32 w0a = lds + L_PF0 + w * 512 + i * 32 + g * 8, w1a = lds + L_PF1 + w * 256 + (((i >> 1) * 32) ^ ((w & 1) << 7)) + g * 8, wxa = lds + L_AUX + w * 128 + i * 8;
    PIN(aa); PIN(aax); PIN(ma); PIN(mr); PIN(w0a); PIN(w1a); PIN(wxa); PIN(qka); PIN(g1);
#pragma unroll
    for (int s = 0; s < 6; ++s) PIN(b1[s]);
    if (!A) { RBAR(); dma_tile(r0, va, vb, s0 + 224 * 384, 128, lds + (8 + w) * 6144); }
    STAMP(1);
    WAITV(6);
    RBAR();
    STAMP(2);
    v4u a0[6]; f4 gg = g1;
#pragma unroll
    for (int s = 0; s < 6; ++s) a0[s] = lds128(((s & 1) ? aax : aa) + (s >> 1) * 2048);
    SB();
#pragma unroll
    for (int s = 0; s < 6; ++s) { gg = mfma(a0[s], a0[s], gg); gg = mfma(b1[s], a0[s], gg); }
    Stat st0 = token_stats(gg, i);
    write_aux(st0, wxa);
    PIN(st0.r); PIN(st0.nrm);
    if (A) {
      v2u fl;
      const u32 vf = WS_FLAG + min(lane, 11) * 8;
      WAITV(0);
      STAMP(3);
      asm volatile("buffer_load_dwordx2 %0, %1, %2, 0 offen sc1" : "=v"(fl) : "v"(vf), "s"(wsr) : "memory");
      v4u a1[6]; gg = g1;
#pragma unroll
      for (int s = 0; s < 6; ++s) a1[s] = lds128(((s & 1) ? aax : aa) + 49152 + (s >> 1) * 2048);
      SB();
#pragma unroll
      for (int s = 0; s < 6; ++s) { gg = mfma(a1[s], a1[s], gg); gg = mfma(b1[s], a1[s], gg); }
      Stat st1 = token_stats(gg, i);
      write_aux(st1, wxa + 1024);
      PIN(st1.r); PIN(st1.nrm);
      BAR();
      STAMP(4);
      f4 sb = {0, 0, 0, 0}, s0_ = {0, 0, 0, 0}, s1_ = {0, 0, 0, 0};
      { v4u qk[6];
#pragma unroll
        for (int s = 0; s < 6; ++s) qk[s] = lds128(qka + s * 1024);
        SB();
#pragma unroll
        for (int s = 0; s < 6; ++s) sb = mfma(qk[s], b1[s], sb);
#pragma unroll
        for (int s = 0; s < 6; ++s) s0_ = mfma(qk[s], a0[s], s0_);
#pragma unroll
        for (int s = 0; s < 6; ++s) s1_ = mfma(qk[s], a1[s], s1_); }
      f4 c0, c1, mx;
#pragma unroll
      for (int k = 0; k < 4; ++k) { const float qs = dpp<0x15F>(sb[k]);
        c0[k] = __builtin_fmaf(0.5f * st0.r, sb[k], __builtin_fmaf(st0.nrm, qs, bias)); c1[k] = __builtin_fmaf(0.5f * st1.r, sb[k], __builtin_fmaf(st1.nrm, qs, bias)); }
      const f4 l0 = logits(s0_, c0, st0.r), l1 = logits(s1_, c1, st1.r);
#pragma unroll
      for (int k = 0; k < 4; ++k) mx[k] = vmax(l0[k], l1[k]);
      fold_max(mx, i, ma);
      BAR();
      { const f4 M = *(LAS f4*)mr; write_p(l0, M, st0.r, w0a, w1a); write_p(l1, M, st1.r, w0a + 4096, w1a + 2048); }
      const float M0 = *(LAS float*)mi;
      BAR();
      STAMP(5);
      asm volatile("s_waitcnt vmcnt(0)" : "+v"(fl) :: "memory");
      POLL_LOOP {
        if (__all((fl.x == tag) & (fl.y == ~tag))) break;
        asm volatile("buffer_load_dwordx2 %0, %1, %2, 0 offen sc1\n\ts_waitcnt vmcnt(0)" : "=&v"(fl) : "v"(vf), "s"(wsr) : "memory"); }
      v4u wf[3][6];
#pragma unroll
      for (int s = 0; s < 6; ++s)
#pragma unroll
        for (int t = 0; t < 3; ++t) wf[t][s] = __builtin_amdgcn_raw_buffer_load_b128(wrs, WS_WVO + ((3 * w + t) * 6 + s) * 1024 + lane * 16, 0, 16);
      f4 ax = {0, 0, 0, 0};
      u32 ua = lds + L_AUX + t4 * 8;
      if (w == 3) {
        v4u x[6], pp[6];
#pragma unroll
        for (int k = 0; k < 6; ++k) { x[k] = cat(ldstr(ua + k * 256), ldstr(ua + k * 256 + 128)); pp[k] = cat(ldstr(pa0 + k * 1024), ldstr(pa0 + k * 1024 + 512)); }
        SB();
#pragma unroll
        for (int k = 0; k < 6; ++k) ax = mfma(x[k], pp[k], ax);
      }
      u32 oa = lds + L_O + lane * 16, mo = WS_M + sidx * 128 + i * 8;
      u32 yo = WS_Y + (((2 * g) * 4 + w) * 2 + (i >> 3)) * (NP * 128) + sidx * 128 + (i & 7) * 16, yo2 = yo + 8 * NP * 128;
      PIN(oa); PIN(mo); PIN(yo); PIN(yo2); PIN(ax);
      STAMP(6);
      BAR();
      v4u x0, x1;
      if (w >= 2) {
        const float M1 = *(LAS float*)mi;
        if (w == 2) { const v2u o = {__float_as_uint(M1), tag}; if (lane < 16) __builtin_amdgcn_raw_buffer_store_b64(o, wrs, mo, 0, 16); }
        else { x0 = cat(ldstr(ua + 12 * 128), ldstr(ua + 13 * 128)); x1 = cat(ldstr(ua + 14 * 128), ldstr(ua + 15 * 128)); ax *= __builtin_amdgcn_exp2f(M0 - M1); PIN(ax); }
      }
      BAR();
      if (w == 3) {
        const v4u p0 = cat(ldstr(pa0 + 12 * 512), ldstr(pa0 + 13 * 512)), p1 = cat(ldstr(pa0 + 14 * 512), ldstr(pa0 + 15 * 512));
        ax = mfma(x0, p0, ax); ax = mfma(x1, p1, ax);
        const v2u o = {__float_as_uint(ax[0] + ax[1]), tag};
        if (lane < 16) __builtin_amdgcn_raw_buffer_store_b64(o, wrs, mo + (WS_L - WS_M), 0, 16);
      }
      BAR();
      STAMP(7);
      f4 ya[3] = {{0, 0, 0, 0}, {0, 0, 0, 0}, {0, 0, 0, 0}}; v4u of[6];
#pragma unroll
      for (int s = 0; s < 6; ++s) of[s] = lds128(oa + s * 1024);
      SB();
#pragma unroll
      for (int s = 0; s < 6; ++s)
#pragma unroll
        for (int t = 0; t < 3; ++t) ya[t] = mfma(of[s], wf[t][s], ya[t]);
#pragma unroll
      for (int e = 0; e < 2; ++e) {
        const u32 c2 = pk(ya[2][2 * e], ya[2][2 * e + 1]);
        const v4u o = {pk(ya[0][2 * e], ya[1][2 * e]), __builtin_amdgcn_perm(tag, c2, 0x05040100u), pk(ya[0][2 * e + 1], ya[1][2 * e + 1]), __builtin_amdgcn_perm(tag, c2, 0x05040302u)};
        __builtin_amdgcn_raw_buffer_store_b128(o, wrs, e ? yo2 : yo, 0, 16);
      }
      STAMP(8);
    } else {
      BAR();
      f4 sb = {0, 0, 0, 0}, sa = {0, 0, 0, 0}, qs; v4u qk[6];
#pragma unroll
      for (int s = 0; s < 6; ++s) qk[s] = lds128(qka + s * 1024);
      SB();
#pragma unroll
      for (int s = 0; s < 6; ++s) sb = mfma(qk[s], b1[s], sb);
#pragma unroll
      for (int s = 0; s < 6; ++s) sa = mfma(qk[s], a0[s], sa);
      f4 c0;
#pragma unroll
      for (int k = 0; k < 4; ++k) { qs[k] = dpp<0x15F>(sb[k]); sb[k] *= 0.5f; c0[k] = __builtin_fmaf(st0.r, sb[k], __builtin_fmaf(st0.nrm, qs[k], bias)); }
      const f4 l0 = logits(sa, c0, st0.r);
      fold_max(l0, i, ma);
      BAR();
      write_p(l0, *(LAS f4*)mr, st0.r, w0a, w1a);
      BAR();
      __builtin_amdgcn_s_setprio(3);
      gg = g1;
#define SEG(sg) do { const v4u a0_ = lds128(aa + 49152 + (sg) * 2048), a1_ = lds128(aax + 49152 + (sg) * 2048); SB(); \
      gg = mfma(a0_, a0_, gg); gg = mfma(b1[2 * (sg)], a0_, gg); gg = mfma(a1_, a1_, gg); gg = mfma(b1[2 * (sg) + 1], a1_, gg); \
      sb = mfma(qk[2 * (sg)], a0_, sb); sb = mfma(qk[2 * (sg) + 1], a1_, sb); PIN(sb); PIN(gg); } while (0)
      WAITV(4); SEG(0);
      WAITV(2); SEG(1);
      WAITV(0);
      STAMP(4);
      SEG(2);
      const Stat st1 = token_stats(gg, i);
      f4 c1;
#pragma unroll
      for (int k = 0; k < 4; ++k) c1[k] = __builtin_fmaf(st1.nrm, qs[k], bias);
      const f4 l1 = logits(sb, c1, st1.r);
      fold_max(l1, i, ma);
      SB();
      write_aux(st1, wxa + 1024);
      STAMP(5);
      BAR();
      write_p(l1, *(LAS f4*)mr, st1.r, w0a + 4096, w1a + 2048);
      STAMP(6);
      BAR();
      __builtin_amdgcn_s_setprio(0);
      RBAR();
    }
  } else {
    const int v = w - 8;
    __builtin_amdgcn_s_setprio(2);
    u32 xa[3], ya_[3];
#pragma unroll
    for (int t = 0; t < 3; ++t) { const int T = 3 * v + t;
      xa[t] = lds + L_F0 + (T >> 2) * 2048 + t4 * 128 + (((T & 3) ^ ((t4 >> 1) & 3)) << 5) + p * 8;
      ya_[t] = lds + L_F1 + g * 3072 + (T >> 2) * 1024 + qq * 128 + (((T & 3) ^ (qq >> 1) ^ ((g & 1) << 1)) << 5) + p * 8; PIN(xa[t]); PIN(ya_[t]); }
    u32 pa1 = lds + L_PF1 + g * 256 + ((g & 1) << 7) + qq * 32 + p * 8, pa1x = pa1 ^ 128;
    u32 qra = lds + L_PF0 + v * 3072 + lane * 16;
    PIN(pa0); PIN(pa1); PIN(pa1x); PIN(qra); PIN(qka); PIN(mi);
    RBAR();
    RBAR();
    qk_request(wsr, v, lane, lds);
    RBAR();
    FENCE();
    v4u xf[3][6], yf[3][2];
#pragma unroll
    for (int t = 0; t < 3; ++t) {
#pragma unroll
      for (int k = 0; k < 4; ++k) xf[t][k] = cat(ldstr(xa[t] + 2 * k * 6144), ldstr(xa[t] + (2 * k + 1) * 6144));
#pragma unroll
      for (int k = 0; k < 2; ++k) yf[t][k] = cat(ldstr(ya_[t] + k * 12288), ldstr((ya_[t] ^ 64) + k * 12288 + 512));
      xa[t] += 8 * 6144;
    }
    WAITV(0);
    STAMP(2);
    v4u qx[3];
    POLL_LOOP {
      bool ok = true;
#pragma unroll
      for (int j = 0; j < 3; ++j) qx[j] = lds128(qra + j * 1024);
#pragma unroll
      for (int j = 0; j < 3; ++j) ok &= (qx[j].y == tag) & (qx[j].w == tag);
      if (__all(ok)) break;
      __builtin_amdgcn_s_sleep(QSLEEP);
      qk_request(wsr, v, lane, lds);
      WAITV(0);
    }
#pragma unroll
    for (int j = 0; j < 3; ++j) { const int pc = 3 * v + j; const v2u o = {qx[j].x, qx[j].z}; *(LAS v2u*)(qka + (pc >> 1) * 1024 + (pc & 1) * 8) = o; }
    STAMP(3);
    BAR();
    __builtin_amdgcn_s_setprio(0);
    RBAR();
    FENCE();
#pragma unroll
    for (int t = 0; t < 3; ++t)
#pragma unroll
      for (int k = 0; k < 2; ++k) xf[t][4 + k] = cat(ldstr(xa[t] + 2 * k * 6144), ldstr(xa[t] + (2 * k + 1) * 6144));
    const float M0 = *(LAS float*)mi;
    BAR();
    f4 acc[3] = {{0, 0, 0, 0}, {0, 0, 0, 0}, {0, 0, 0, 0}};
    { v4u pf[6], pg[3];
#pragma unroll
      for (int k = 0; k < 6; ++k) pf[k] = cat(ldstr(pa0 + k * 1024), ldstr(pa0 + k * 1024 + 512));
#pragma unroll
      for (int k = 0; k < 3; ++k) pg[k] = cat(ldstr(pa1 + k * 1024), ldstr(pa1x + k * 1024));
      SB();
#pragma unroll
      for (int k = 0; k < 6; ++k)
#pragma unroll
        for (int t = 0; t < 3; ++t) acc[t] = mfma(xf[t][k], pf[k], acc[t]);
#pragma unroll
      for (int k = 0; k < 3; ++k)
#pragma unroll
        for (int t = 0; t < 3; ++t) acc[t] = mfma(yf[t][k & 1], pg[k], acc[t]); }
    u32 ow[3];
#pragma unroll
    for (int t = 0; t < 3; ++t) { const int T = 3 * v + t; ow[t] = lds + L_O + lane * 16 + (T >> 1) * 1024 + (T & 1) * 8; PIN(ow[t]); PIN(acc[t]); }
    STAMP(6);
    RBAR();
    FENCE();
    const float M1 = *(LAS float*)mi;
#pragma unroll
    for (int t = 0; t < 3; ++t)
#pragma unroll
      for (int k = 0; k < 2; ++k) xf[t][k] = cat(ldstr(xa[t] + (4 + 2 * k) * 6144), ldstr(xa[t] + (5 + 2 * k) * 6144));
    { const float al = __builtin_amdgcn_exp2f(M0 - M1);
#pragma unroll
      for (int t = 0; t < 3; ++t) { acc[t] *= al; PIN(acc[t]); } }
    BAR();
    { const v4u p0 = cat(ldstr(pa0 + 6144), ldstr(pa0 + 6144 + 512)), p1 = cat(ldstr(pa0 + 7168), ldstr(pa0 + 7168 + 512)), p2 = cat(ldstr(pa1 + 3072), ldstr(pa1x + 3072));
      SB();
#pragma unroll
      for (int t = 0; t < 3; ++t) {
        acc[t] = mfma(xf[t][0], p0, acc[t]); acc[t] = mfma(xf[t][1], p1, acc[t]); acc[t] = mfma(yf[t][1], p2, acc[t]); }
#pragma unroll
      for (int t = 0; t < 3; ++t) { const v2u o = {pk(acc[t][0], acc[t][1]), pk(acc[t][2], acc[t][3])}; *(LAS v2u*)ow[t] = o; } }
    BAR();
  }
  return tag;
}

DEVI void reducer(int R, char* ws, u32 tag, u32 lds STAMP_ARG) {
  const int tid = threadIdx.x, lane = tid & 63, w = __builtin_amdgcn_readfirstlane(tid >> 6);
  const v4u wsr = mkrs(ws, WS_END);
  constexpr u32 L_RM = L_JUNK, L_RP = L_PF1;
  const int qp = R >> 3, i8 = tid & 7;
  if (w >= 4 && w < 11) {
    const int p0 = (w - 4) * 32 + (lane >> 3);
    u32 mo = WS_M + p0 * 128 + qp * 16, lo_ = mo + (WS_L - WS_M), yo = WS_Y + (R * NP + p0) * 128 + i8 * 16;
    u32 rpa = lds + L_RP + (w - 4) * 256 + lane * 4;
    v4u m[4], l[4], y[4];
    const u32 t16 = tag & 0xffffu;
#define LOAD4(d, o, W) asm volatile("buffer_load_dwordx4 %0, %4, %5, 0 offen sc1\n\tbuffer_load_dwordx4 %1, %4, %5, 0 offen offset:1024 sc1\n\tbuffer_load_dwordx4 %2, %4, %5, 0 offen offset:2048 sc1\n\tbuffer_load_dwordx4 %3, %4, %5, 0 offen offset:3072 sc1" W \
                   : "=&v"(d[0]), "=&v"(d[1]), "=&v"(d[2]), "=&v"(d[3]) : "v"(o), "s"(wsr) : "memory")
    POLL_LOOP {
      LOAD4(m, mo, "\n\ts_waitcnt vmcnt(0)");
      bool ok = true;
#pragma unroll
      for (int k = 0; k < 4; ++k) ok &= (m[k].y == tag) & (m[k].w == tag);
      if (__all(ok)) break;
      __builtin_amdgcn_s_sleep(RSLEEP);
    }
    LOAD4(y, yo, ""); LOAD4(l, lo_, "");
    STAMP(10);
    {
      const float xa = vmax(vmax(__uint_as_float(m[0].x), __uint_as_float(m[1].x)), vmax(__uint_as_float(m[2].x), __uint_as_float(m[3].x)));
      const float xb = vmax(vmax(__uint_as_float(m[0].z), __uint_as_float(m[1].z)), vmax(__uint_as_float(m[2].z), __uint_as_float(m[3].z)));
      float x = (i8 & 1) ? xb : xa;
      x = vmax(x, dpp<0x128>(x));
      if ((lane & 14) == 0) __builtin_amdgcn_ds_fmaxf((LAS float*)(lds + L_RM + (i8 & 1) * 4), x, 0, 0, false);
      FENCE();
      if (lane == 0) __hip_atomic_fetch_add((LAS u32*)(lds + L_RM + 8), 1u, __ATOMIC_RELAXED, __HIP_MEMORY_SCOPE_WORKGROUP);
    }
    v4u rm;
    POLL_LOOP { FENCE(); const u32 cnt = *(volatile LAS u32*)(lds + L_RM + 8); rm = *(volatile LAS v4u*)(lds + L_RM); if (__builtin_amdgcn_readfirstlane(cnt) == 7u) break; }
    float ea[4], eb[4];
#pragma unroll
    for (int k = 0; k < 4; ++k) { ea[k] = __builtin_amdgcn_exp2f(__uint_as_float(m[k].x) - __uint_as_float(rm.x)); eb[k] = __builtin_amdgcn_exp2f(__uint_as_float(m[k].z) - __uint_as_float(rm.y)); PIN(ea[k]); PIN(eb[k]); }
    asm volatile("s_waitcnt vmcnt(0)" : "+v"(y[0]), "+v"(y[1]), "+v"(y[2]), "+v"(y[3]), "+v"(l[0]), "+v"(l[1]), "+v"(l[2]), "+v"(l[3]) :: "memory");
    POLL_LOOP {
      bool ok = true;
#pragma unroll
      for (int k = 0; k < 4; ++k) ok &= ((y[k].y >> 16) == t16) & ((y[k].w >> 16) == t16) & (l[k].y == tag) & (l[k].w == tag);
      if (__all(ok)) break;
      LOAD4(y, yo, "\n\ts_waitcnt vmcnt(0)"); LOAD4(l, lo_, "\n\ts_waitcnt vmcnt(0)");
    }
    STAMP(11);
    float v[8] = {0, 0, 0, 0, 0, 0, 0, 0};
#pragma unroll
    for (int k = 0; k < 4; ++k) {
      v[0] = __builtin_fmaf(ea[k], blo(y[k].x), v[0]); v[1] = __builtin_fmaf(ea[k], bhi(y[k].x), v[1]); v[2] = __builtin_fmaf(ea[k], blo(y[k].y), v[2]); v[3] = __builtin_fmaf(ea[k], __uint_as_float(l[k].x), v[3]);
      v[4] = __builtin_fmaf(eb[k], blo(y[k].z), v[4]); v[5] = __builtin_fmaf(eb[k], bhi(y[k].z), v[5]); v[6] = __builtin_fmaf(eb[k], blo(y[k].w), v[6]); v[7] = __builtin_fmaf(eb[k], __uint_as_float(l[k].z), v[7]); }
#pragma unroll
    for (int j = 0; j < 4; ++j) { auto s = __builtin_amdgcn_permlane32_swap(__float_as_uint(v[j]), __float_as_uint(v[j + 4]), false, false); v[j] = __uint_as_float(s[0]) + __uint_as_float(s[1]); }
#pragma unroll
    for (int j = 0; j < 2; ++j) { auto s = __builtin_amdgcn_permlane16_swap(__float_as_uint(v[j]), __float_as_uint(v[j + 2]), false, false); v[j] = __uint_as_float(s[0]) + __uint_as_float(s[1]); }
    const float e0 = v[0] + dpp<0x128>(v[0]), e1 = v[1] + dpp<0x128>(v[1]);
    *(LAS float*)rpa = (lane & 8) ? e1 : e0;
    BAR();
  } else if (w == 11) {
    const int b5 = lane >> 5, t = (lane >> 3) & 3;
    const int oidx = (2 * qp + b5) * C + 48 * ((R >> 1) & 3) + 16 * min(t, 2) + 8 * (R & 1) + i8;
    float qv = blo(((const GAS u16*)karg(3))[oidx]);
    u32 rpa = lds + L_RP + lane * 4;
    GAS u16* op = (GAS u16*)karg(8) + oidx;
    PIN(qv); PIN(rpa); PIN(op);
    BAR();
    float x[7];
#pragma unroll
    for (int k = 0; k < 7; ++k) x[k] = *(LAS float*)(rpa + k * 256);
    SB();
    const float s = ((x[0] + x[1]) + (x[2] + x[3])) + ((x[4] + x[5]) + x[6]);
    const float da = __int_as_float(__builtin_amdgcn_readlane(__float_as_int(s), 31)), db = __int_as_float(__builtin_amdgcn_readlane(__float_as_int(s), 63));
    const float o = qv + s * __builtin_amdgcn_rcpf(b5 ? db : da);
    if (t < 3) *op = (u16)(pk(o, 0.f) & 0xffffu);
    STAMP(12);
  } else {
    BAR();
    if (R == 0 && tid == 0) *(GAS u32*)ws = tag;
  }
}

DEVI void code_prefetch(u64 pc0, int piece, int lane, u32 lds) { dma16(mkrs((const void*)(pc0 & ~255ull), CODE_KB * 1024), lane * 16, piece * 1024, lds + L_PRE); }

__global__ void __launch_bounds__(768, 3) k_fused(const u16* __restrict__ f0, const u16* __restrict__ f1, char* ws, const u16* __restrict__ q, const u16* __restrict__ wq, const u16* __restrict__ wk,
                                                 const u16* __restrict__ wv, const u16* __restrict__ wo, u16* out) {
  __shared__ __attribute__((aligned(256))) char smem[L_TOTAL];
  const u32 lds = (u32)(size_t)(LAS char*)smem;
  STAMP_DECL
  STAMP(0);
  u64 ka0, ka1, ka2;
  asm volatile("s_load_dwordx2 %0, %3, 0x0\n\ts_load_dwordx2 %1, %3, 0x8\n\ts_load_dwordx2 %2, %3, 0x10" : "=&s"(ka0), "=&s"(ka1), "=&s"(ka2) : "s"(__builtin_amdgcn_kernarg_segment_ptr()) : "memory");
  u64 pc0; asm volatile("s_getpc_b64 %0" : "=s"(pc0));
  const int b = blockIdx.x, c = b & 7, rk = b >> 3;
  const int tid = threadIdx.x, lane = tid & 63;
  const u32 r8 = lane >> 3, lo = lane & 7;
  const int w = __builtin_amdgcn_readfirstlane(tid >> 6);
  { const int cp = c == 0 ? rk - 15 : rk; if (w == 4 && (unsigned)cp < CODE_KB) code_prefetch(pc0, cp, lane, lds); }
  const u32 va = r8 * 384 + ((lo ^ (r8 & 6)) << 4);
  if (tid == 767) { const v4u z = {0xff800000u, 0xff800000u, 0u, 0u}; *(LAS v4u*)(lds + L_JUNK) = z; }
  u32 tag;
  if (c != 0) {
    const int sidx = rk * 7 + c - 1;
    u32 s1 = (sidx * 56 + 7 * w) * 384, v1 = va ^ ((w & 1) << 6), l1 = lds + L_F1 + w * 3072;
    asm volatile("" : "+s"(s1), "+s"(l1)); PIN(v1);
    u64 pf0, pf1, pws;
    asm volatile("s_waitcnt lgkmcnt(0)\n\ts_mov_b64 %0, %3\n\ts_mov_b64 %1, %4\n\ts_mov_b64 %2, %5" : "=&s"(pf0), "=&s"(pf1), "=&s"(pws) : "s"(ka0), "s"(ka1), "s"(ka2) : "memory");
    if (w < 4) {
      const v4u r1 = mkrs((const void*)pf1, F1B);
      if (lane < 56) {
#pragma unroll
        for (int h = 0; h < 2; ++h)
#pragma unroll
          for (int sg = 0; sg < 3; ++sg) dma16(r1, v1, s1 + h * (28 * 384) + sg * 128, l1 + h * 12288 + sg * 1024);
      }
    } else RBAR();
    const v4u r0 = mkrs((const void*)pf0, F0B);
    const u32 vb = lane >= 48 ? 0x80000000u : va + 3072;
    const u32 s0 = ((sidx & ~1) * 224 + (sidx & 1) * 112 + 14 * w) * 384;
    if (w < 8) dma_tile(r0, va, vb, s0, 128, lds + w * 6144);
    if (w < 4) RBAR();
    STAMP(13);
    tag = streamer(sidx, r0, s0, va, vb, (char*)pws, lds STAMP_PASS);
  } else {
    const bool hq = rk < 3;
    u64 pq, pw, pv, pws;
    asm volatile("s_load_dwordx2 %0, %4, 0x18\n\ts_load_dwordx2 %1, %4, %5\n\ts_load_dwordx2 %2, %4, %6\n\ts_waitcnt lgkmcnt(0)\n\ts_mov_b64 %3, %9" : "=&s"(pq), "=&s"(pw), "=&s"(pv), "=&s"(pws)
                 : "s"(__builtin_amdgcn_kernarg_segment_ptr()), "s"(rk >= 15 ? 32u + 8u * (w & 3) : hq ? 32u : 48u), "s"(hq ? 40u : 56u), "s"(ka0), "s"(ka1), "s"(ka2) : "memory");
    STAMP(2);
    const v4u rs = mkrs((const void*)pw, WB);
    if (rk >= 15) {
      const u32 so = ((rk - 15) * 3 + (w >> 2)) * 1024;
      dma16(rs, lane * 16, so, lds + L_PRE); dma16(rs, lane * 16, so + 51 * 1024, lds + L_PRE);
      WAITV(0);
      return;
    }
    const u32 vt = va ^ (64u & (r8 << 4));
    if (hq && w < 4) { const v4u rq = mkrs((const void*)pq, 6144u); dma16(rq, lane * 16, w * 1536, lds + L_F1 + w * 2048); dma16(rq, lane * 16, w * 1536 + 1024, lds + L_F1 + w * 2048 + 1024); }
    dma_tile(rs, hq ? vt : va, (hq ? vt ^ 64 : va) + 3072, w * 6144, 128, lds + w * 6144);
    if (w < 4) dma_tile(mkrs((const void*)pv, WB), hq ? va : vt, (hq ? va : vt ^ 64) + 3072, hq ? (4 * rk + w) * 6144 : w * (48 * 384) + ((rk - 3) >> 2) * 128, hq ? 128 : 6144, lds + (12 + w) * 6144);
    tag = get_tag(req_epoch((char*)pws));
    if (hq) helper_qk(rk, (char*)pws, tag, lds STAMP_PASS);
    else helper_wvo(rk - 3, (char*)pws, tag, lds STAMP_PASS);
  }
  STAMP(9);
  if (b < 64) reducer(b, karg(2), tag, lds STAMP_PASS);
}

extern "C" void kernel_launch(void* const* d_in, const int* in_sizes, int n_in, void* d_out, int out_size, void* d_ws, size_t ws_size, hipStream_t stream) {
  k_fused<<<256, 768, 0, stream>>>((const u16*)d_in[1], (const u16*)d_in[2], (char*)d_ws, (const u16*)d_in[0], (const u16*)d_in[3], (const u16*)d_in[4], (const u16*)d_in[5], (const u16*)d_in[6], (u16*)d_out);
}
```
